```python
import torch

def _softmax(x, axis=-1):
    return torch.softmax(x, dim=axis)
def _gelu(x):
    return torch.nn.functional.gelu(x, approximate="tanh")
def _silu(x):
    return torch.nn.functional.silu(x)
def _sigmoid(x):
    return torch.sigmoid(x)
def _layernorm(x, axis=-1, eps=1e-5):
    m = x.mean(dim=axis, keepdim=True)
    v = x.var(dim=axis, keepdim=True, unbiased=False)
    return (x - m) / torch.sqrt(v + eps)
def _scatter_add(tgt, idx, src, axis=0):
    return tgt.clone().index_add_(axis, idx, src)
def _segment_sum(src, seg, n):
    out = torch.zeros((n,) + src.shape[1:], dtype=src.dtype)
    return out.index_add_(0, seg, src)
def _pad(x, pw):
    flat = tuple(v for pair in reversed(pw) for v in pair)
    return torch.nn.functional.pad(x, flat)

kernel_name = 'interleave_layers__sliding_local_dilated__mixer_s_mlp__prompt'

def setup_inputs():

    _w = (lambda a: (lambda t: t.to(torch.bfloat16) if t.is_floating_point() else t)(torch.as_tensor(a)))
    def _A_setup():
        B, S, D = 1, 2048, 448
        return {
            "x": _w((lambda *s: torch.randn(*s, dtype=torch.bfloat16) * 0.05)(B, S, D)),
            "wqkv": _w((lambda *s: torch.randn(*s, dtype=torch.bfloat16) * 0.1)(D, 3 * D)),
            "wo": _w((lambda *s: torch.randn(*s, dtype=torch.bfloat16) * 0.1)(D, D)),
        }
    _A_in = _A_setup()

    def _B_setup():
        B, S, D, F = 1, 2048, 448, 1792
        return {
            "x":  _w((lambda *s: torch.randn(*s, dtype=torch.bfloat16) * 0.05)(B, S, D)),
            "w1": _w((lambda *s: torch.randn(*s, dtype=torch.bfloat16) * 3.2)(D, D) * (D ** -0.5)),
            "w2": _w((lambda *s: torch.randn(*s, dtype=torch.bfloat16) * 3.2)(D, D) * (D ** -0.5)),
            "w3": _w((lambda *s: torch.randn(*s, dtype=torch.bfloat16) * 3.2)(D, F) * (D ** -0.5)),
            "w4": _w((lambda *s: torch.randn(*s, dtype=torch.bfloat16) * 3.2)(F, D) * (F ** -0.5)),
        }
    _B_in = _B_setup()

    return {
        "A_x": _A_in["x"],
        "A_wqkv": _A_in["wqkv"],
        "A_wo": _A_in["wo"],
        "B_x": _B_in["x"],
        "B_w1": _B_in["w1"],
        "B_w2": _B_in["w2"],
        "B_w3": _B_in["w3"],
        "B_w4": _B_in["w4"],
    }

def reference(A_x, A_wqkv, A_wo, B_x, B_w1, B_w2, B_w3, B_w4):
    def _A_ref(x, wqkv, wo):
        B, S, D = x.shape
        H = 16; Dh = D // H
        W, dil = 8, 2
        qkv = (lambda x, *d: x.permute(*d))((x @ wqkv).reshape(B, S, 3, H, Dh), 2, 0, 3, 1, 4)
        q, k, v = qkv[0], qkv[1], qkv[2]
        scale = Dh ** -0.5
        pos = torch.arange(S)
        off = torch.arange(W) * dil
        idx = pos[:, None] - off[None, :]
        mask = idx >= 0
        idx_c = torch.where(mask, idx, 0).reshape(-1)
        kg = k[:, :, idx_c, :].reshape(B, H, S, W, Dh)
        vg = v[:, :, idx_c, :].reshape(B, H, S, W, Dh)
        logits = torch.einsum("bhsd,bhswd->bhsw", q, kg) * scale
        logits = torch.where(mask[None, None, :, :], logits, -1e9)
        attn = _softmax(logits, axis=-1)
        o = torch.einsum("bhsw,bhswd->bhsd", attn, vg)
        return (lambda x, *d: x.permute(*d))(o, 0, 2, 1, 3).reshape(B, S, D) @ wo

    def _B_ref(x, w1, w2, w3, w4):
        B, S, D = x.shape
        h = _gelu(_layernorm(x, axis=-1) @ w1)
        c = D // 4
        g0 = _pad(h[:, :, 0*c:1*c], ((0, 0), (1, 0), (0, 0)))[:, :S, :]
        g1 = _pad(h[:, :, 1*c:2*c], ((0, 0), (0, 1), (0, 0)))[:, 1:, :]
        g2 = _pad(h[:, :, 2*c:3*c], ((0, 0), (2, 0), (0, 0)))[:, :S, :]
        g3 = _pad(h[:, :, 3*c:   ], ((0, 0), (0, 2), (0, 0)))[:, 2:, :]
        hs = torch.concatenate([g0, g1, g2, g3], axis=-1) @ w2
        x1 = x + hs
        h2 = _gelu(_layernorm(x1, axis=-1) @ w3) @ w4
        return x1 + h2

    x = A_x
    x = x + _A_ref(_layernorm(x, axis=-1), A_wqkv, A_wo)
    x = x + _B_ref(_layernorm(x, axis=-1), B_w1, B_w2, B_w3, B_w4)
    x = x + _A_ref(_layernorm(x, axis=-1), A_wqkv, A_wo)
    x = x + _B_ref(_layernorm(x, axis=-1), B_w1, B_w2, B_w3, B_w4)
    return x
```

```cpp
#include <hip/hip_runtime.h>
#include <stdint.h>

namespace {

typedef __attribute__((ext_vector_type(8))) short bf16x8;
typedef __attribute__((ext_vector_type(4))) short s16x4;
typedef __attribute__((ext_vector_type(4))) float f32x4;
typedef __attribute__((ext_vector_type(4))) unsigned u32x4;
typedef __attribute__((ext_vector_type(2))) unsigned u32x2;
typedef __attribute__((ext_vector_type(2))) __bf16 bfx2;
typedef __attribute__((address_space(3))) s16x4 lds_s16x4;

constexpr int NT = 512;
constexpr int SEQ = 2048, DM = 448, FF = 1792;
constexpr int GC = 56;
constexpr unsigned ROWB = DM * 2;

constexpr unsigned SZ_D = SEQ * DM * 2u;
constexpr unsigned SZ_F = SEQ * FF * 2u;
constexpr unsigned LAYER_SZ = 5u * SZ_D + SZ_F;
constexpr unsigned O_OFF = 0, X2_OFF = SZ_D, Y_OFF = 2u * SZ_D, H_OFF = 3u * SZ_D, X1_OFF = 4u * SZ_D, G_OFF = 5u * SZ_D;
constexpr unsigned XO_OFF = 2u * LAYER_SZ;
constexpr unsigned WS_USED = 2u * LAYER_SZ + SZ_D;

constexpr int AUX_LD = 16;
constexpr int AUX_ST = 16;

struct Params {
  const uint16_t* x;
  const uint16_t* wqkv;
  const uint16_t* wo;
  const uint16_t* w1;
  const uint16_t* w2;
  const uint16_t* w3;
  const uint16_t* w4;
  uint16_t* out;
  char* ws;
};

typedef __amdgpu_buffer_rsrc_t rsrc_t;
__device__ __forceinline__ rsrc_t mk_rsrc(const void* p, unsigned bytes) {
  return __builtin_amdgcn_make_buffer_rsrc((void*)p, (short)0, (int)bytes, 0x00020000);
}

__device__ __forceinline__ float bf_lo(unsigned u) { return __uint_as_float(u << 16); }
__device__ __forceinline__ float bf_hi(unsigned u) { return __uint_as_float(u & 0xffff0000u); }
__device__ __forceinline__ unsigned pack2(float lo, float hi) {
  bfx2 v; v[0] = (__bf16)lo; v[1] = (__bf16)hi;
  return __builtin_bit_cast(unsigned, v);
}
__device__ __forceinline__ float rbf(float f) { return bf_lo(pack2(f, 0.f)); }

__device__ __forceinline__ float red8(float v) {
  v += __int_as_float(__builtin_amdgcn_update_dpp(0, __float_as_int(v), 0xB1, 0xF, 0xF, false));
  v += __int_as_float(__builtin_amdgcn_update_dpp(0, __float_as_int(v), 0x4E, 0xF, 0xF, false));
  v += __int_as_float(__builtin_amdgcn_update_dpp(0, __float_as_int(v), 0x141, 0xF, 0xF, false));
  return v;
}

__device__ __forceinline__ void ln_row(u32x4 (&a)[7]) {
  const bfx2 ones = {(__bf16)1.0f, (__bf16)1.0f};
  float s = 0.f;
#pragma unroll
  for (int j = 0; j < 7; ++j)
#pragma unroll
    for (int e = 0; e < 4; ++e) {
      const unsigned u = a[j][e];
      s = __builtin_amdgcn_fdot2_f32_bf16(__builtin_bit_cast(bfx2, u), ones, s, false);
    }
  s = red8(s);
  const float mean = s * (1.0f / 448.0f);
  const float mb = rbf(mean);
  float q = 0.f;
#pragma unroll
  for (int j = 0; j < 7; ++j)
#pragma unroll
    for (int e = 0; e < 4; ++e) {
      float d0 = bf_lo(a[j][e]) - mb, d1 = bf_hi(a[j][e]) - mb;
      q = fmaf(d0, d0, q); q = fmaf(d1, d1, q);
    }
  q = red8(q);
  const float dm = mean - mb;
  float var = q * (1.0f / 448.0f) - dm * dm;
  var = fmaxf(var, 0.f);
  const float vb = rbf(var);
  const float sd = rbf(sqrtf(rbf(vb + 1e-5f)));
  const float inv = 1.0f / sd;
#pragma unroll
  for (int j = 0; j < 7; ++j)
#pragma unroll
    for (int e = 0; e < 4; ++e) {
      unsigned dpk = pack2(bf_lo(a[j][e]) - mb, bf_hi(a[j][e]) - mb);
      a[j][e] = pack2(bf_lo(dpk) * inv, bf_hi(dpk) * inv);
    }
}

__device__ __forceinline__ unsigned a_off(int row, int chunk) {
  return (unsigned)row * ROWB + (unsigned)((chunk ^ ((row >> 1) & 7)) << 4);
}

__device__ __forceinline__ f32x4 mfma16(bf16x8 w, bf16x8 a, f32x4 c) {
  return __builtin_amdgcn_mfma_f32_16x16x32_bf16(w, a, c, 0, 0, 0);
}

template <int NB>
__device__ __forceinline__ bf16x8 w_frag(const char* sW, int kpos, int nb, unsigned wlane) {
  unsigned off = (unsigned)((4 * kpos * NB + nb) * 256) + wlane;
  s16x4 lo = __builtin_amdgcn_ds_read_tr16_b64_v4i16((lds_s16x4*)(sW + off));
  s16x4 hi = __builtin_amdgcn_ds_read_tr16_b64_v4i16((lds_s16x4*)(sW + (off ^ 128u)));
  bf16x8 r;
  r[0] = lo[0]; r[1] = lo[1]; r[2] = lo[2]; r[3] = lo[3];
  r[4] = hi[0]; r[5] = hi[1]; r[6] = hi[2]; r[7] = hi[3];
  return r;
}
template <int NB>
__device__ __forceinline__ unsigned w_lane_off(int lane) {
  int fq = lane >> 4, i = lane & 15, q = i >> 2, p = i & 3;
  int r0 = q ^ ((fq & 1) << 2);
  return (unsigned)(fq * NB * 256 + r0 * 32 + p * 8);
}

__device__ __forceinline__ bf16x8 a_frag(const char* sA, int mt, int ks, int lane) {
  int fr = lane & 15, fq = lane >> 4;
  int row = 16 * mt + fr;
  return *reinterpret_cast<const bf16x8*>(sA + a_off(row, 4 * ks + fq));
}

__device__ __forceinline__ float gelu_f(float x) {
  const float c0 = -2.0f * 0.7978845608028654f * 1.4426950408889634f;
  const float c1 = c0 * 0.044715f;
  float t = x * x;
  float w = fmaf(t, c1, c0);
  float e = __builtin_amdgcn_exp2f(x * w);
  return x * __builtin_amdgcn_rcpf(1.0f + e);
}

__device__ __forceinline__ void wg_map(int bid, int& rb, int& g) {
  int x = bid & 7, i = bid >> 3;
  rb = x * 4 + (i >> 3);
  g = i & 7;
}

template <int NB, typename ColFn>
__device__ __forceinline__ void dma_w_instr(const uint16_t* W, int ldw, int k0, int instr, char* dst_kstep, int lane, ColFn colfn) {
  int b = 4 * instr + (lane >> 4);
  int kbl = b / NB, nb = b - kbl * NB;
  int s16 = lane & 15, rs = s16 >> 1, hf = s16 & 1;
  int r = rs ^ ((kbl & 1) << 2);
  const uint16_t* src = W + (size_t)(k0 + 8 * kbl + r) * ldw + colfn(nb, hf);
  __builtin_amdgcn_global_load_lds((const unsigned*)src, (unsigned*)(dst_kstep + instr * 1024 + lane * 16), 16, 0, 0);
}

template <typename OffFn>
__device__ __forceinline__ void load_row_chunks(rsrc_t rs, u32x4 (&a)[7], int c, OffFn offfn) {
#pragma unroll
  for (int j = 0; j < 7; ++j) a[j] = __builtin_amdgcn_raw_buffer_load_b128(rs, offfn(c + 8 * j), 0, AUX_LD);
}
__device__ __forceinline__ void store_row_chunks(char* sA, const u32x4 (&a)[7], int row, int c) {
#pragma unroll
  for (int j = 0; j < 7; ++j) *reinterpret_cast<u32x4*>(sA + a_off(row, c + 8 * j)) = a[j];
}

__device__ __forceinline__ void gemm64_resident(const char* sA, const char* sW, int wave, int lane, f32x4 (&acc)[2][2]) {
  const int wm = wave >> 1, wn = wave & 1;
  const unsigned wl = w_lane_off<4>(lane);
#pragma unroll
  for (int ks = 0; ks < 14; ++ks) {
    bf16x8 a0 = a_frag(sA, 2 * wm, ks, lane), a1 = a_frag(sA, 2 * wm + 1, ks, lane);
    bf16x8 w0 = w_frag<4>(sW, ks, 2 * wn, wl), w1 = w_frag<4>(sW, ks, 2 * wn + 1, wl);
    acc[0][0] = mfma16(w0, a0, acc[0][0]);
    acc[0][1] = mfma16(w1, a0, acc[0][1]);
    acc[1][0] = mfma16(w0, a1, acc[1][0]);
    acc[1][1] = mfma16(w1, a1, acc[1][1]);
  }
}

__device__ __forceinline__ void stage_tile(char* sT, unsigned stride, int mt, int nt, int lane, unsigned lo, unsigned hi) {
  int fr = lane & 15, fq = lane >> 4;
  u32x2 v; v[0] = lo; v[1] = hi;
  *reinterpret_cast<u32x2*>(sT + (unsigned)(16 * mt + fr) * stride + (unsigned)(16 * nt + 4 * fq) * 2u) = v;
}

constexpr unsigned ST64 = 144;

template <typename ColFn>
__device__ __forceinline__ void dma_w_full64(const uint16_t* W, int ldw, char* sW, int wave, int lane, ColFn colfn) {
#pragma unroll
  for (int i = 0; i < 7; ++i) {
    int j = wave + 8 * i;
    int ks = j >> 2, ins = j & 3;
    dma_w_instr<4>(W, ldw, 32 * ks, ins, sW + ks * 4096, lane, colfn);
  }
}

template <int MODE>
__device__ __forceinline__ void stage_small(const Params& p, int layer, int rb, int g, char* smem) {
  const int t = threadIdx.x, wave = t >> 6, lane = t & 63;
  char* sA = smem;
  char* sW = smem + 64 * ROWB;
  char* ws = p.ws + (size_t)layer * LAYER_SZ;
  const uint16_t* W = MODE == 2 ? p.wo : (MODE == 3 ? p.w1 : p.w2);
  auto colfn = [&](int nb, int hf) { int c = GC * g + 16 * nb + 8 * hf; return c > DM - 8 ? DM - 8 : c; };
  dma_w_full64(W, DM, sW, wave, lane, colfn);

  const int row = t >> 3, c = t & 7;
  const int grow = 64 * rb + row;
  u32x4 a[7];
  if (MODE == 2) {
    rsrc_t rs = mk_rsrc(ws + O_OFF, SZ_D);
    load_row_chunks(rs, a, c, [&](int ch) { return (unsigned)grow * ROWB + (unsigned)ch * 16u; });
  } else if (MODE == 3) {
    rsrc_t rs = mk_rsrc(ws + X2_OFF, SZ_D);
    load_row_chunks(rs, a, c, [&](int ch) { return (unsigned)grow * ROWB + (unsigned)ch * 16u; });
    ln_row(a);
    rsrc_t ry = mk_rsrc(ws + Y_OFF, SZ_D);
#pragma unroll
    for (int j = 0; j < 7; ++j) {
      int ch = c + 8 * j;
      if (ch / 7 == g) __builtin_amdgcn_raw_buffer_store_b128(a[j], ry, (unsigned)grow * ROWB + (unsigned)ch * 16u, 0, AUX_ST);
    }
    ln_row(a);
  } else {
    rsrc_t rs = mk_rsrc(ws + H_OFF, SZ_D);
    load_row_chunks(rs, a, c, [&](int ch) {
      int q = ch / 14;
      int dlt = (q == 0) ? -1 : (q == 1) ? 1 : (q == 2) ? -2 : 2;
      int sr = grow + dlt;
      return (sr >= 0 && sr < SEQ) ? (unsigned)sr * ROWB + (unsigned)ch * 16u : 0xFFFFFF00u;
    });
  }
  store_row_chunks(sA, a, row, c);
  asm volatile("s_waitcnt vmcnt(0)" ::: "memory");
  __syncthreads();

  f32x4 acc[2][2] = {};
  if (wave < 4) gemm64_resident(sA, sW, wave, lane, acc);
  __syncthreads();
  char* sT = smem;
  if (wave < 4) {
    const int wm = wave >> 1, wn = wave & 1;
#pragma unroll
    for (int i = 0; i < 2; ++i)
#pragma unroll
      for (int j = 0; j < 2; ++j) {
        f32x4 v = acc[i][j];
        unsigned lo = pack2(v[0], v[1]), hi = pack2(v[2], v[3]);
        if (MODE == 3) {
          lo = pack2(gelu_f(bf_lo(lo)), gelu_f(bf_hi(lo)));
          hi = pack2(gelu_f(bf_lo(hi)), gelu_f(bf_hi(hi)));
        }
        stage_tile(sT, ST64, 2 * wm + i, 2 * wn + j, lane, lo, hi);
      }
  }
  __syncthreads();
  if (t < 448) {
    int r = t / 7, ch = t - 7 * r;
    unsigned goff = (unsigned)(64 * rb + r) * ROWB + (unsigned)(GC * g) * 2u + (unsigned)ch * 16u;
    u32x4 v = *reinterpret_cast<const u32x4*>(sT + (unsigned)r * ST64 + (unsigned)ch * 16u);
    if (MODE == 3) {
      __builtin_amdgcn_raw_buffer_store_b128(v, mk_rsrc(ws + H_OFF, SZ_D), goff, 0, AUX_ST);
    } else {
      u32x4 rsd;
      if (MODE == 2) {
        if (layer == 0) rsd = __builtin_amdgcn_raw_buffer_load_b128(mk_rsrc(p.x, SZ_D), goff, 0, AUX_LD);
        else rsd = __builtin_amdgcn_raw_buffer_load_b128(mk_rsrc(p.ws + XO_OFF, SZ_D), goff, 0, AUX_LD);
      } else {
        rsd = __builtin_amdgcn_raw_buffer_load_b128(mk_rsrc(ws + Y_OFF, SZ_D), goff, 0, AUX_LD);
      }
      u32x4 o;
#pragma unroll
      for (int e = 0; e < 4; ++e) o[e] = pack2(bf_lo(rsd[e]) + bf_lo(v[e]), bf_hi(rsd[e]) + bf_hi(v[e]));
      __builtin_amdgcn_raw_buffer_store_b128(o, mk_rsrc(ws + (MODE == 2 ? X2_OFF : X1_OFF), SZ_D), goff, 0, AUX_ST);
    }
  }
}

template <int N> __device__ __forceinline__ void wait_vm() { asm volatile("s_waitcnt vmcnt(%0)" ::"n"(N) : "memory"); }

template <int NB, typename ColFn>
__device__ __forceinline__ void ring_issue(const uint16_t* W, int ldw, int k0, char* slot, int lw, int lane, ColFn colfn) {
  constexpr int C = (NB + 3) / 4;
#pragma unroll
  for (int i = 0; i < C; ++i) {
    int ins = lw + 4 * i;
    ins = ins > NB - 1 ? NB - 1 : ins;
    dma_w_instr<NB>(W, ldw, k0, ins, slot, lane, colfn);
  }
}

constexpr int S5_NB = 14, S5_R = 7;
constexpr unsigned ST224 = 464;
__device__ __forceinline__ void stage_s5(const Params& p, int layer, int rb, int g, char* smem) {
  const int t = threadIdx.x, wave = t >> 6, lane = t & 63;
  char* sA = smem;
  char* sW = smem + 64 * ROWB;
  char* ws = p.ws + (size_t)layer * LAYER_SZ;
  auto colfn = [&](int nb, int hf) { return 224 * g + 16 * nb + 8 * hf; };
  constexpr int C = (S5_NB + 3) / 4;
  if (wave >= 4) {
#pragma unroll
    for (int s = 0; s < S5_R; ++s) ring_issue<S5_NB>(p.w3, FF, 32 * s, sW + s * (S5_NB * 1024), wave - 4, lane, colfn);
  }
  {
    const int row = t >> 3, c = t & 7;
    const int grow = 64 * rb + row;
    u32x4 a[7];
    rsrc_t rs = mk_rsrc(ws + X1_OFF, SZ_D);
    load_row_chunks(rs, a, c, [&](int ch) { return (unsigned)grow * ROWB + (unsigned)ch * 16u; });
    ln_row(a);
    store_row_chunks(sA, a, row, c);
  }
  f32x4 acc[2][7] = {};
  const int wm = (wave >> 1) & 1, wn = wave & 1;
  const unsigned wl = w_lane_off<S5_NB>(lane);
  for (int s = 0; s < 14; ++s) {
    if (wave >= 4) {
      if (s + S5_R - 2 <= 13) wait_vm<C*(S5_R - 2)>(); else wait_vm<0>();
    }
    __builtin_amdgcn_s_waitcnt(0xc07f);
    __builtin_amdgcn_s_barrier();
    if (wave >= 4) {
      if (s >= 1 && s - 1 + S5_R < 14) ring_issue<S5_NB>(p.w3, FF, 32 * (s - 1 + S5_R), sW + ((s - 1) % S5_R) * (S5_NB * 1024), wave - 4, lane, colfn);
    } else {
      const char* slot = sW + (s % S5_R) * (S5_NB * 1024);
      bf16x8 a0 = a_frag(sA, 2 * wm, s, lane), a1 = a_frag(sA, 2 * wm + 1, s, lane);
#pragma unroll
      for (int j = 0; j < 7; ++j) {
        bf16x8 w = w_frag<S5_NB>(slot, 0, 7 * wn + j, wl);
        acc[0][j] = mfma16(w, a0, acc[0][j]);
        acc[1][j] = mfma16(w, a1, acc[1][j]);
      }
    }
  }
  __syncthreads();
  char* sT = smem;
  if (wave < 4) {
#pragma unroll
    for (int i = 0; i < 2; ++i)
#pragma unroll
      for (int j = 0; j < 7; ++j) {
        f32x4 v = acc[i][j];
        unsigned lo = pack2(v[0], v[1]), hi = pack2(v[2], v[3]);
        lo = pack2(gelu_f(bf_lo(lo)), gelu_f(bf_hi(lo)));
        hi = pack2(gelu_f(bf_lo(hi)), gelu_f(bf_hi(hi)));
        stage_tile(sT, ST224, 2 * wm + i, 7 * wn + j, lane, lo, hi);
      }
  }
  __syncthreads();
  rsrc_t rg = mk_rsrc(ws + G_OFF, SZ_F);
#pragma unroll
  for (int i = 0; i < 4; ++i) {
    int it = t + NT * i;
    if (it < 64 * 28) {
      int r = it / 28, ch = it - 28 * r;
      u32x4 v = *reinterpret_cast<const u32x4*>(sT + (unsigned)r * ST224 + (unsigned)ch * 16u);
      __builtin_amdgcn_raw_buffer_store_b128(v, rg, (unsigned)(64 * rb + r) * (FF * 2u) + (unsigned)(224 * g) * 2u + (unsigned)ch * 16u, 0, AUX_ST);
    }
  }
}

constexpr int S6_R = 24;
__device__ __forceinline__ void stage_s6(const Params& p, int layer, int rb, int g, char* smem) {
  const int t = threadIdx.x, wave = t >> 6, lane = t & 63;
  char* sA = smem;
  char* sW = smem + 64 * ROWB;
  char* ws = p.ws + (size_t)layer * LAYER_SZ;
  auto colfn = [&](int nb, int hf) { int c = GC * g + 16 * nb + 8 * hf; return c > DM - 8 ? DM - 8 : c; };
  if (wave >= 4) {
#pragma unroll
    for (int s = 0; s < S6_R; ++s) ring_issue<4>(p.w4, DM, 32 * s, sW + s * 4096, wave - 4, lane, colfn);
  }
  rsrc_t rg = mk_rsrc(ws + G_OFF, SZ_F);
  u32x4 a[2][7];
  const int arow = (t >> 3) & 31, ac = t & 7;
  auto issue_a = [&](int sc) {
#pragma unroll
    for (int h = 0; h < 2; ++h) {
      unsigned base = (unsigned)(64 * rb + arow + 32 * h) * (FF * 2u) + (unsigned)sc * ROWB;
      load_row_chunks(rg, a[h], ac, [&](int ch) { return base + (unsigned)ch * 16u; });
    }
  };
  if (wave < 4) {
    issue_a(0);
    store_row_chunks(sA, a[0], arow, ac);
    store_row_chunks(sA, a[1], arow + 32, ac);
    issue_a(1);
  }
  f32x4 acc[2][2] = {};
  const int wm = (wave >> 1) & 1, wn = wave & 1;
  const unsigned wl = w_lane_off<4>(lane);
  for (int s = 0; s < 56; ++s) {
    const int ksl = s % 14;
    if (ksl == 0 && s > 0) {
      __builtin_amdgcn_s_waitcnt(0xc07f);
      __builtin_amdgcn_s_barrier();
      if (wave < 4) {
        store_row_chunks(sA, a[0], arow, ac);
        store_row_chunks(sA, a[1], arow + 32, ac);
        if (s / 14 + 1 < 4) issue_a(s / 14 + 1);
      }
    }
    if (wave >= 4) {
      if (s + S6_R - 2 <= 55) wait_vm<(S6_R - 2)>(); else wait_vm<0>();
    }
    __builtin_amdgcn_s_waitcnt(0xc07f);
    __builtin_amdgcn_s_barrier();
    if (wave >= 4) {
      if (s >= 1 && s - 1 + S6_R < 56) ring_issue<4>(p.w4, DM, 32 * (s - 1 + S6_R), sW + ((s - 1) % S6_R) * 4096, wave - 4, lane, colfn);
    } else {
      const char* slot = sW + (s % S6_R) * 4096;
      bf16x8 a0 = a_frag(sA, 2 * wm, ksl, lane), a1 = a_frag(sA, 2 * wm + 1, ksl, lane);
      bf16x8 w0 = w_frag<4>(slot, 0, 2 * wn, wl), w1 = w_frag<4>(slot, 0, 2 * wn + 1, wl);
      acc[0][0] = mfma16(w0, a0, acc[0][0]);
      acc[0][1] = mfma16(w1, a0, acc[0][1]);
      acc[1][0] = mfma16(w0, a1, acc[1][0]);
      acc[1][1] = mfma16(w1, a1, acc[1][1]);
    }
  }
  __syncthreads();
  char* sT = smem;
  if (wave < 4) {
#pragma unroll
    for (int i = 0; i < 2; ++i)
#pragma unroll
      for (int j = 0; j < 2; ++j) {
        f32x4 v = acc[i][j];
        stage_tile(sT, ST64, 2 * wm + i, 2 * wn + j, lane, pack2(v[0], v[1]), pack2(v[2], v[3]));
      }
  }
  __syncthreads();
  if (t < 448) {
    int r = t / 7, ch = t - 7 * r;
    unsigned goff = (unsigned)(64 * rb + r) * ROWB + (unsigned)(GC * g) * 2u + (unsigned)ch * 16u;
    u32x4 v = *reinterpret_cast<const u32x4*>(sT + (unsigned)r * ST64 + (unsigned)ch * 16u);
    u32x4 x1 = __builtin_amdgcn_raw_buffer_load_b128(mk_rsrc(ws + X1_OFF, SZ_D), goff, 0, AUX_LD);
    u32x4 x2 = __builtin_amdgcn_raw_buffer_load_b128(mk_rsrc(ws + X2_OFF, SZ_D), goff, 0, AUX_LD);
    u32x4 o;
#pragma unroll
    for (int e = 0; e < 4; ++e) {
      unsigned rr = pack2(bf_lo(x1[e]) + bf_lo(v[e]), bf_hi(x1[e]) + bf_hi(v[e]));
      o[e] = pack2(bf_lo(x2[e]) + bf_lo(rr), bf_hi(x2[e]) + bf_hi(rr));
    }
    if (layer == 0) __builtin_amdgcn_raw_buffer_store_b128(o, mk_rsrc(p.ws + XO_OFF, SZ_D), goff, 0, AUX_ST);
    else __builtin_amdgcn_raw_buffer_store_b128(o, mk_rsrc(p.out, SZ_D), goff, 0, 0);
  }
}

constexpr int S1_NB = 12, S1_R = 7;
__device__ __forceinline__ void stage_s1(const Params& p, int layer, int rb, int g, char* smem) {
  const int t = threadIdx.x, wave = t >> 6, lane = t & 63;
  char* sA = smem;
  char* sW = smem + 80 * ROWB;
  char* ws = p.ws + (size_t)layer * LAYER_SZ;
  auto colfn = [&](int nb, int hf) {
    int part = nb >> 2;
    int c = GC * g + 16 * (nb & 3) + 8 * hf;
    c = c > DM - 8 ? DM - 8 : c;
    return part * DM + c;
  };
  constexpr int C = (S1_NB + 3) / 4;
  if (wave >= 4) {
#pragma unroll
    for (int s = 0; s < S1_R; ++s) ring_issue<S1_NB>(p.wqkv, 3 * DM, 32 * s, sW + s * (S1_NB * 1024), wave - 4, lane, colfn);
  }
  {
    rsrc_t rs = layer == 0 ? mk_rsrc(p.x, SZ_D) : mk_rsrc(p.ws + XO_OFF, SZ_D);
    const int c = t & 7;
#pragma unroll
    for (int ps = 0; ps < 2; ++ps) {
      int row = (t >> 3) + 64 * ps;
      if (row < 80) {
        int grow = 64 * rb - 16 + row;
        grow = grow < 0 ? 0 : grow;
        u32x4 a[7];
        load_row_chunks(rs, a, c, [&](int ch) { return (unsigned)grow * ROWB + (unsigned)ch * 16u; });
        ln_row(a);
        store_row_chunks(sA, a, row, c);
      }
    }
  }
  f32x4 aq[4] = {}, ak[5] = {}, av[5] = {};
  const int nt = wave & 3;
  const unsigned wl = w_lane_off<S1_NB>(lane);
  for (int s = 0; s < 14; ++s) {
    if (wave >= 4) {
      if (s + S1_R - 2 <= 13) wait_vm<C*(S1_R - 2)>(); else wait_vm<0>();
    }
    __builtin_amdgcn_s_waitcnt(0xc07f);
    __builtin_amdgcn_s_barrier();
    if (wave >= 4) {
      if (s >= 1 && s - 1 + S1_R < 14) ring_issue<S1_NB>(p.wqkv, 3 * DM, 32 * (s - 1 + S1_R), sW + ((s - 1) % S1_R) * (S1_NB * 1024), wave - 4, lane, colfn);
    } else {
      const char* slot = sW + (s % S1_R) * (S1_NB * 1024);
      bf16x8 wq = w_frag<S1_NB>(slot, 0, nt, wl), wk = w_frag<S1_NB>(slot, 0, 4 + nt, wl), wv = w_frag<S1_NB>(slot, 0, 8 + nt, wl);
#pragma unroll
      for (int m = 0; m < 5; ++m) {
        bf16x8 af = a_frag(sA, m, s, lane);
        if (m > 0) aq[m - 1] = mfma16(wq, af, aq[m - 1]);
        ak[m] = mfma16(wk, af, ak[m]);
        av[m] = mfma16(wv, af, av[m]);
      }
    }
  }
  __syncthreads();
  char* sQ = smem;
  char* sK = smem + 64 * ST64;
  char* sV = smem + 144 * ST64;
  if (wave < 4) {
#pragma unroll
    for (int m = 0; m < 4; ++m) stage_tile(sQ, ST64, m, nt, lane, pack2(aq[m][0], aq[m][1]), pack2(aq[m][2], aq[m][3]));
#pragma unroll
    for (int m = 0; m < 5; ++m) {
      stage_tile(sK, ST64, m, nt, lane, pack2(ak[m][0], ak[m][1]), pack2(ak[m][2], ak[m][3]));
      stage_tile(sV, ST64, m, nt, lane, pack2(av[m][0], av[m][1]), pack2(av[m][2], av[m][3]));
    }
  }
  __syncthreads();
  char* sO = smem + 224 * ST64;
  {
    const int sub = t & 3, pr = t >> 2, row = pr >> 1, head = pr & 1;
    const int spos = 64 * rb + row;
    const u32x2* qp = reinterpret_cast<const u32x2*>(sQ + row * ST64 + head * 56);
    u32x2 qv[7];
#pragma unroll
    for (int i = 0; i < 7; ++i) qv[i] = qp[i];
    float lg[2];
#pragma unroll
    for (int ww = 0; ww < 2; ++ww) {
      int w = 2 * sub + ww;
      const u32x2* kp = reinterpret_cast<const u32x2*>(sK + (row + 16 - 2 * w) * ST64 + head * 56);
      float d = 0.f;
#pragma unroll
      for (int i = 0; i < 7; ++i) {
        u32x2 kv = kp[i];
        d = fmaf(bf_lo(qv[i][0]), bf_lo(kv[0]), d); d = fmaf(bf_hi(qv[i][0]), bf_hi(kv[0]), d);
        d = fmaf(bf_lo(qv[i][1]), bf_lo(kv[1]), d); d = fmaf(bf_hi(qv[i][1]), bf_hi(kv[1]), d);
      }
      lg[ww] = rbf(rbf(d) * 0.18898223650461362f);
    }
    float l8[8];
#define QBC(j, ctl) \
    l8[2 * j] = __int_as_float(__builtin_amdgcn_mov_dpp(__float_as_int(lg[0]), ctl, 0xF, 0xF, false)); \
    l8[2 * j + 1] = __int_as_float(__builtin_amdgcn_mov_dpp(__float_as_int(lg[1]), ctl, 0xF, 0xF, false));
    QBC(0, 0x00) QBC(1, 0x55) QBC(2, 0xAA) QBC(3, 0xFF)
#undef QBC
    float mx = -3.0e38f;
#pragma unroll
    for (int w = 0; w < 8; ++w) mx = (spos - 2 * w >= 0) ? fmaxf(mx, l8[w]) : mx;
    float ex[8], sum = 0.f;
#pragma unroll
    for (int w = 0; w < 8; ++w) {
      ex[w] = (spos - 2 * w >= 0) ? __expf(l8[w] - mx) : 0.f;
      sum += ex[w];
    }
    const float rs = 1.0f / sum;
    float o[7] = {};
#pragma unroll
    for (int w = 0; w < 8; ++w) {
      float pw = rbf(ex[w] * rs);
      const uint16_t* vp = reinterpret_cast<const uint16_t*>(sV + (row + 16 - 2 * w) * ST64 + head * 56 + sub * 14);
      bool valid = (spos - 2 * w >= 0);
#pragma unroll
      for (int d = 0; d < 7; ++d) {
        float vv = __uint_as_float((unsigned)vp[d] << 16);
        o[d] = valid ? fmaf(pw, vv, o[d]) : o[d];
      }
    }
    uint16_t* op = reinterpret_cast<uint16_t*>(sO + row * 128 + head * 56 + sub * 14);
#pragma unroll
    for (int d = 0; d < 7; ++d) op[d] = (uint16_t)(pack2(o[d], 0.f) & 0xffffu);
  }
  __syncthreads();
  if (t < 448) {
    int r = t / 7, ch = t - 7 * r;
    unsigned goff = (unsigned)(64 * rb + r) * ROWB + (unsigned)(GC * g) * 2u + (unsigned)ch * 16u;
    u32x4 v = *reinterpret_cast<const u32x4*>(sO + r * 128 + ch * 16);
    __builtin_amdgcn_raw_buffer_store_b128(v, mk_rsrc(ws + O_OFF, SZ_D), goff, 0, AUX_ST);
  }
}

template <int STAGE>
__global__ __launch_bounds__(NT, 2) void stage_kernel(Params p, int layer) {
  extern __shared__ __attribute__((aligned(16))) char smem[];
  int rb, g;
  wg_map(blockIdx.x, rb, g);
  if (STAGE == 1) stage_s1(p, layer, rb, g, smem);
  else if (STAGE == 2) stage_small<2>(p, layer, rb, g, smem);
  else if (STAGE == 3) stage_small<3>(p, layer, rb, g, smem);
  else if (STAGE == 4) stage_small<4>(p, layer, rb, g, smem);
  else if (STAGE == 5) stage_s5(p, layer, rb, g, smem);
  else stage_s6(p, layer, rb, g, smem);
}

constexpr unsigned LDS_S1 = 80 * ROWB + S1_R * S1_NB * 1024;
constexpr unsigned LDS_SM = 64 * ROWB + 14 * 4096;
constexpr unsigned LDS_S5 = 64 * ROWB + S5_R * S5_NB * 1024;
constexpr unsigned LDS_S6 = 64 * ROWB + S6_R * 4096;

}

extern "C" void kernel_launch(void* const* d_in, const int* in_sizes, int n_in,
                              void* d_out, int out_size, void* d_ws, size_t ws_size,
                              hipStream_t stream) {
  Params p;
  p.x = (const uint16_t*)d_in[0];
  p.wqkv = (const uint16_t*)d_in[1];
  p.wo = (const uint16_t*)d_in[2];
  p.w1 = (const uint16_t*)d_in[4];
  p.w2 = (const uint16_t*)d_in[5];
  p.w3 = (const uint16_t*)d_in[6];
  p.w4 = (const uint16_t*)d_in[7];
  p.out = (uint16_t*)d_out;
  p.ws = (char*)d_ws;
  (void)hipFuncSetAttribute((const void*)stage_kernel<1>, hipFuncAttributeMaxDynamicSharedMemorySize, LDS_S1);
  (void)hipFuncSetAttribute((const void*)stage_kernel<2>, hipFuncAttributeMaxDynamicSharedMemorySize, LDS_SM);
  (void)hipFuncSetAttribute((const void*)stage_kernel<3>, hipFuncAttributeMaxDynamicSharedMemorySize, LDS_SM);
  (void)hipFuncSetAttribute((const void*)stage_kernel<4>, hipFuncAttributeMaxDynamicSharedMemorySize, LDS_SM);
  (void)hipFuncSetAttribute((const void*)stage_kernel<5>, hipFuncAttributeMaxDynamicSharedMemorySize, LDS_S5);
  (void)hipFuncSetAttribute((const void*)stage_kernel<6>, hipFuncAttributeMaxDynamicSharedMemorySize, LDS_S6);
  for (int layer = 0; layer < 2; ++layer) {
    stage_kernel<1><<<256, NT, LDS_S1, stream>>>(p, layer);
    stage_kernel<2><<<256, NT, LDS_SM, stream>>>(p, layer);
    stage_kernel<3><<<256, NT, LDS_SM, stream>>>(p, layer);
    stage_kernel<4><<<256, NT, LDS_SM, stream>>>(p, layer);
    stage_kernel<5><<<256, NT, LDS_S5, stream>>>(p, layer);
    stage_kernel<6><<<256, NT, LDS_S6, stream>>>(p, layer);
  }
}
```

```cpp
#include <hip/hip_runtime.h>
#include <stdint.h>

namespace {

typedef __attribute__((ext_vector_type(8))) short bf16x8;
typedef __attribute__((ext_vector_type(4))) short s16x4;
typedef __attribute__((ext_vector_type(4))) float f32x4;
typedef __attribute__((ext_vector_type(2))) float f32x2;
typedef __attribute__((ext_vector_type(4))) unsigned u32x4;
typedef __attribute__((ext_vector_type(2))) unsigned u32x2;
typedef __attribute__((ext_vector_type(2))) __bf16 bfx2;
typedef __attribute__((address_space(3))) s16x4 lds_s16x4;

constexpr int NT = 512;
constexpr int SEQ = 2048, DM = 448, FF = 1792;
constexpr int GC = 56;
constexpr unsigned ROWB = DM * 2;

constexpr unsigned SZ_D = SEQ * DM * 2u;
constexpr unsigned SZ_F = SEQ * FF * 2u;
constexpr unsigned LAYER_SZ = 5u * SZ_D + SZ_F;
constexpr unsigned O_OFF = 0, X2_OFF = SZ_D, Y_OFF = 2u * SZ_D, H_OFF = 3u * SZ_D, X1_OFF = 4u * SZ_D, G_OFF = 5u * SZ_D;
constexpr unsigned XO_OFF = 2u * LAYER_SZ;
constexpr unsigned WS_USED = 2u * LAYER_SZ + SZ_D;
constexpr unsigned FLAG_OFF = WS_USED;
constexpr unsigned FLAG_BYTES = 12u * 32u * 128u;
constexpr unsigned WI_OFF = FLAG_OFF + FLAG_BYTES;
constexpr unsigned WI_QKV = 0;
constexpr unsigned WI_O = WI_QKV + 8u * 12u * 14u * 1024u;
constexpr unsigned WI_1 = WI_O + 8u * 4u * 14u * 1024u;
constexpr unsigned WI_2 = WI_1 + 8u * 4u * 14u * 1024u;
constexpr unsigned WI_3 = WI_2 + 8u * 4u * 14u * 1024u;
constexpr unsigned WI_4 = WI_3 + 8u * 14u * 14u * 1024u;
constexpr unsigned WI_BYTES = WI_4 + 8u * 4u * 56u * 1024u;

constexpr int AUX_SC1 = 16;
#define ABL 0

struct Params {
  const uint16_t* x;
  const uint16_t* wqkv;
  const uint16_t* wo;
  const uint16_t* w1;
  const uint16_t* w2;
  const uint16_t* w3;
  const uint16_t* w4;
  uint16_t* out;
  char* ws;
};

#define STAMP(st, i) do {} while (0)

typedef __amdgpu_buffer_rsrc_t rsrc_t;
__device__ __forceinline__ rsrc_t mk_rsrc(const void* p, unsigned bytes) {
  return __builtin_amdgcn_make_buffer_rsrc((void*)p, (short)0, (int)bytes, 0x00020000);
}
__device__ __forceinline__ u32x4 ld_sc1(rsrc_t rs, unsigned off) { return __builtin_amdgcn_raw_buffer_load_b128(rs, off, 0, AUX_SC1); }
__device__ __forceinline__ void st_sc1(rsrc_t rs, unsigned off, u32x4 v) { __builtin_amdgcn_raw_buffer_store_b128(v, rs, off, 0, AUX_SC1); }

__device__ __forceinline__ float bf_lo(unsigned u) { return __uint_as_float(u << 16); }
__device__ __forceinline__ float bf_hi(unsigned u) { return __uint_as_float(u & 0xffff0000u); }
__device__ __forceinline__ f32x2 bf_up(unsigned u) { f32x2 r; r[0] = bf_lo(u); r[1] = bf_hi(u); return r; }
__device__ __forceinline__ unsigned pack2(float lo, float hi) {
  bfx2 v; v[0] = (__bf16)lo; v[1] = (__bf16)hi;
  return __builtin_bit_cast(unsigned, v);
}
__device__ __forceinline__ unsigned pack2v(f32x2 v) { return pack2(v[0], v[1]); }
__device__ __forceinline__ float rbf(float f) { return bf_lo(pack2(f, 0.f)); }
__device__ __forceinline__ float dot2(unsigned a, unsigned b, float c) {
  return __builtin_amdgcn_fdot2_f32_bf16(__builtin_bit_cast(bfx2, a), __builtin_bit_cast(bfx2, b), c, false);
}

__device__ __forceinline__ float red8(float v) {
  v += __int_as_float(__builtin_amdgcn_update_dpp(0, __float_as_int(v), 0xB1, 0xF, 0xF, false));
  v += __int_as_float(__builtin_amdgcn_update_dpp(0, __float_as_int(v), 0x4E, 0xF, 0xF, false));
  v += __int_as_float(__builtin_amdgcn_update_dpp(0, __float_as_int(v), 0x141, 0xF, 0xF, false));
  return v;
}
__device__ __forceinline__ float red4(float v) {
  v += __int_as_float(__builtin_amdgcn_update_dpp(0, __float_as_int(v), 0xB1, 0xF, 0xF, false));
  v += __int_as_float(__builtin_amdgcn_update_dpp(0, __float_as_int(v), 0x4E, 0xF, 0xF, false));
  return v;
}

__device__ __forceinline__ void ln_row(u32x4 (&a)[7]) {
  const unsigned ones = 0x3F803F80u;
  float s = 0.f;
#pragma unroll
  for (int j = 0; j < 7; ++j)
#pragma unroll
    for (int e = 0; e < 4; ++e) { const unsigned u = a[j][e]; s = dot2(u, ones, s); }
  s = red8(s);
  const float mean = s * (1.0f / 448.0f);
  const float mb = rbf(mean);
  const f32x2 mb2 = {mb, mb};
  f32x2 q2 = {0.f, 0.f};
#pragma unroll
  for (int j = 0; j < 7; ++j)
#pragma unroll
    for (int e = 0; e < 4; ++e) { const unsigned u = a[j][e]; f32x2 d = bf_up(u) - mb2; q2 = d * d + q2; }
  float q = red8(q2[0] + q2[1]);
  const float dm = mean - mb;
  float var = fmaxf(q * (1.0f / 448.0f) - dm * dm, 0.f);
  const float sd = rbf(sqrtf(rbf(rbf(var) + 1e-5f)));
  const float inv = 1.0f / sd;
  const f32x2 inv2 = {inv, inv};
#pragma unroll
  for (int j = 0; j < 7; ++j)
#pragma unroll
    for (int e = 0; e < 4; ++e) {
      const unsigned u = a[j][e];
      const unsigned dpk = pack2v(bf_up(u) - mb2);
      a[j][e] = pack2v(bf_up(dpk) * inv2);
    }
}

__device__ __forceinline__ float gelu_f(float x) {
  const float c0 = -2.0f * 0.7978845608028654f * 1.4426950408889634f;
  const float c1 = c0 * 0.044715f;
  float w = fmaf(x * x, c1, c0);
  float e = __builtin_amdgcn_exp2f(x * w);
  return x * __builtin_amdgcn_rcpf(1.0f + e);
}
__device__ __forceinline__ unsigned gelu_pk(unsigned u) { return pack2(gelu_f(bf_lo(u)), gelu_f(bf_hi(u))); }

__device__ __forceinline__ f32x4 mfma16(bf16x8 w, bf16x8 a, f32x4 c) {
  return __builtin_amdgcn_mfma_f32_16x16x32_bf16(w, a, c, 0, 0, 0);
}

__device__ __forceinline__ unsigned a_off(int row, int chunk) {
  return (unsigned)row * ROWB + (unsigned)((chunk ^ ((row >> 1) & 7)) << 4);
}
__device__ __forceinline__ bf16x8 a_frag(const char* sA, int mt, int ks, int lane) {
  return *reinterpret_cast<const bf16x8*>(sA + a_off(16 * mt + (lane & 15), 4 * ks + (lane >> 4)));
}

template <int NB>
__device__ __forceinline__ bf16x8 w_frag(const char* sW, int kpos, int nb, unsigned wlane) {
  unsigned off = (unsigned)((4 * kpos * NB + nb) * 256) + wlane;
  s16x4 lo = __builtin_amdgcn_ds_read_tr16_b64_v4i16((lds_s16x4*)(sW + off));
  s16x4 hi = __builtin_amdgcn_ds_read_tr16_b64_v4i16((lds_s16x4*)(sW + (off ^ 128u)));
  bf16x8 r;
  r[0] = lo[0]; r[1] = lo[1]; r[2] = lo[2]; r[3] = lo[3];
  r[4] = hi[0]; r[5] = hi[1]; r[6] = hi[2]; r[7] = hi[3];
  return r;
}
template <int NB>
__device__ __forceinline__ unsigned w_lane_off(int lane) {
  int fq = lane >> 4, i = lane & 15, q = i >> 2, p = i & 3;
  int r0 = q ^ ((fq & 1) << 2);
  return (unsigned)(fq * NB * 256 + r0 * 32 + p * 8);
}
template <int NB, typename ColFn>
__device__ __forceinline__ unsigned w_piece_off(int ldw, int piece, int lane, ColFn colfn) {
  int b = 4 * piece + (lane >> 4);
  int kbl = b / NB, nb = b - kbl * NB;
  int s16 = lane & 15, rs = s16 >> 1, hf = s16 & 1;
  int r = rs ^ ((kbl & 1) << 2);
  return (unsigned)((8 * kbl + r) * ldw + colfn(nb, hf));
}

__device__ __forceinline__ void stage_tile(char* sT, unsigned stride, int mt, int nt, int lane, f32x4 v) {
  u32x2 w; w[0] = pack2(v[0], v[1]); w[1] = pack2(v[2], v[3]);
  *reinterpret_cast<u32x2*>(sT + (unsigned)(16 * mt + (lane & 15)) * stride + (unsigned)(16 * nt + 4 * (lane >> 4)) * 2u) = w;
}
constexpr unsigned ST64 = 144, ST224 = 464;

__device__ __forceinline__ unsigned* cnt_ptr(const Params& p, int si, int rb) {
  return reinterpret_cast<unsigned*>(p.ws + FLAG_OFF + ((unsigned)si * 32u + (unsigned)rb) * 128u);
}
__device__ __forceinline__ void wait_blocks(const Params& p, int si, int rb_lo, int rb_hi) {
  if (threadIdx.x == 0) {
    for (int r = rb_lo; r <= rb_hi; ++r) {
      if (r < 0 || r > 31) continue;
      unsigned* c = cnt_ptr(p, si, r);
      unsigned spins = 0;
      while (__hip_atomic_load(c, __ATOMIC_RELAXED, __HIP_MEMORY_SCOPE_AGENT) < 8u) {
        __builtin_amdgcn_s_sleep(1);
        if (++spins > (1u << 22)) break;
      }
    }
  }
  __syncthreads();
}
__device__ __forceinline__ void signal_block(const Params& p, int si, int rb) {
  asm volatile("s_waitcnt vmcnt(0)" ::: "memory");
  __syncthreads();
  if (threadIdx.x == 0) __hip_atomic_fetch_add(cnt_ptr(p, si, rb), 1u, __ATOMIC_RELAXED, __HIP_MEMORY_SCOPE_AGENT);
}

#define STAGE_IDS() \
  int t = threadIdx.x; asm volatile("" : "+v"(t)); asm volatile("" : "+s"(rb), "+s"(g)); \
  const int wave = t >> 6, lane = t & 63;

__device__ __forceinline__ bf16x8 ldw_frag(const char* p) { return *reinterpret_cast<const bf16x8*>(p); }

template <int MODE>
__device__ __forceinline__ void stage_small(const Params& p, int layer, int rb, int g, char* smem) {
  STAMP(MODE, 0);
  STAGE_IDS();
  char* sA = smem;
  char* ws = p.ws + (size_t)layer * LAYER_SZ;
  bf16x8 wf[14];
  if (wave < 4) {
    const char* wsrc = p.ws + WI_OFF + (MODE == 2 ? WI_O : (MODE == 3 ? WI_1 : WI_2)) + ((unsigned)g * 4u + wave) * 14u * 1024u + lane * 16;
#pragma unroll
    for (int ks = 0; ks < 14; ++ks) wf[ks] = ldw_frag(wsrc + ks * 1024);
  }
  if (MODE == 4) wait_blocks(p, layer * 6 + 2, rb - 1, rb + 1);
  else wait_blocks(p, layer * 6 + MODE - 2, rb, rb);

  const int row = t >> 3, c = t & 7;
  const int grow = 64 * rb + row;
  u32x4 a[7];
  if (MODE == 4) {
    rsrc_t rs = mk_rsrc(ws + H_OFF, SZ_D);
#pragma unroll
    for (int j = 0; j < 7; ++j) {
      int ch = c + 8 * j, q = ch / 14;
      int sr = grow + ((q == 0) ? -1 : (q == 1) ? 1 : (q == 2) ? -2 : 2);
      a[j] = ld_sc1(rs, (sr >= 0 && sr < SEQ) ? (unsigned)sr * ROWB + (unsigned)ch * 16u : 0xFFFFFF00u);
    }
  } else {
    rsrc_t rs = mk_rsrc(ws + (MODE == 2 ? O_OFF : X2_OFF), SZ_D);
#pragma unroll
    for (int j = 0; j < 7; ++j) a[j] = ld_sc1(rs, (unsigned)grow * ROWB + (unsigned)(c + 8 * j) * 16u);
  }
  const int er = t / 7, ech = t - 7 * er;
  const unsigned goff = (unsigned)(64 * rb + er) * ROWB + (unsigned)(GC * g) * 2u + (unsigned)ech * 16u;
  u32x4 rsd = {0u, 0u, 0u, 0u};
  if (MODE != 3 && t < 448) {
    if (MODE == 2) rsd = ld_sc1(layer == 0 ? mk_rsrc(p.x, SZ_D) : mk_rsrc(p.ws + XO_OFF, SZ_D), goff);
    else rsd = ld_sc1(mk_rsrc(ws + Y_OFF, SZ_D), goff);
  }
  if (MODE == 3) {
    ln_row(a);
    rsrc_t ry = mk_rsrc(ws + Y_OFF, SZ_D);
#pragma unroll
    for (int j = 0; j < 7; ++j) {
      int ch = c + 8 * j;
      if (ch / 7 == g) st_sc1(ry, (unsigned)grow * ROWB + (unsigned)ch * 16u, a[j]);
    }
    ln_row(a);
  }
#pragma unroll
  for (int j = 0; j < 7; ++j) *reinterpret_cast<u32x4*>(sA + a_off(row, c + 8 * j)) = a[j];
  STAMP(MODE, 2);
  __syncthreads();
  STAMP(MODE, 3);

  f32x4 acc[4] = {};
  if (wave < 4) {
    bf16x8 af[2][4];
#pragma unroll
    for (int m = 0; m < 4; ++m) af[0][m] = a_frag(sA, m, 0, lane);
#pragma unroll
    for (int ks = 0; ks < 14; ++ks) {
      if (ks + 1 < 14) {
#pragma unroll
        for (int m = 0; m < 4; ++m) af[(ks + 1) & 1][m] = a_frag(sA, m, ks + 1, lane);
      }
      __builtin_amdgcn_sched_barrier(0);
#pragma unroll
      for (int m = 0; m < 4; ++m) acc[m] = mfma16(wf[ks], af[ks & 1][m], acc[m]);
      __builtin_amdgcn_sched_barrier(0);
    }
  }
  __syncthreads();
  STAMP(MODE, 4);
  char* sT = smem;
  if (wave < 4) {
#pragma unroll
    for (int m = 0; m < 4; ++m) stage_tile(sT, ST64, m, wave, lane, acc[m]);
  }
  __syncthreads();
  if (t < 448) {
    u32x4 v = *reinterpret_cast<const u32x4*>(sT + (unsigned)er * ST64 + (unsigned)ech * 16u);
    u32x4 o;
    if (MODE == 3) {
#pragma unroll
      for (int e = 0; e < 4; ++e) o[e] = gelu_pk(v[e]);
      st_sc1(mk_rsrc(ws + H_OFF, SZ_D), goff, o);
    } else {
#pragma unroll
      for (int e = 0; e < 4; ++e) o[e] = pack2v(bf_up(rsd[e]) + bf_up(v[e]));
      st_sc1(mk_rsrc(ws + (MODE == 2 ? X2_OFF : X1_OFF), SZ_D), goff, o);
    }
  }
  signal_block(p, layer * 6 + MODE - 1, rb);
  STAMP(MODE, 5);
}

__device__ __forceinline__ void stage_s5(const Params& p, int layer, int rb, int g, char* smem) {
  STAMP(5, 0);
  STAGE_IDS();
  constexpr int PF = 5;
  char* sA = smem;
  char* ws = p.ws + (size_t)layer * LAYER_SZ;
  const int nt0 = wave < 2 ? 4 * wave : 8 + 3 * (wave - 2);
  const bool four = wave < 2;
  const char* wsrc = p.ws + WI_OFF + WI_3 + ((unsigned)g * 14u + (unsigned)(wave & 3 ) * 0u + (unsigned)nt0) * 14u * 1024u + lane * 16;
  bf16x8 wf[PF + 1][4];
  auto issue_w = [&](int ks, bf16x8 (&r)[4]) {
#pragma unroll
    for (int j = 0; j < 4; ++j) if (j < 3 || four) r[j] = ldw_frag(wsrc + (j * 14 + ks) * 1024);
  };
  if (wave < 4) {
#pragma unroll
    for (int d = 0; d < PF; ++d) issue_w(d, wf[d]);
  }
  wait_blocks(p, layer * 6 + 3, rb, rb);
  {
    const int row = t >> 3, c = t & 7;
    u32x4 a[7];
    rsrc_t rs = mk_rsrc(ws + X1_OFF, SZ_D);
#pragma unroll
    for (int j = 0; j < 7; ++j) a[j] = ld_sc1(rs, (unsigned)(64 * rb + row) * ROWB + (unsigned)(c + 8 * j) * 16u);
    ln_row(a);
#pragma unroll
    for (int j = 0; j < 7; ++j) *reinterpret_cast<u32x4*>(sA + a_off(row, c + 8 * j)) = a[j];
  }
  STAMP(5, 1);
  __syncthreads();
  f32x4 acc[4][4] = {};
  if (wave < 4) {
    bf16x8 af[2][4];
#pragma unroll
    for (int m = 0; m < 4; ++m) af[0][m] = a_frag(sA, m, 0, lane);
#pragma unroll
    for (int ks = 0; ks < 14; ++ks) {
      if (ks + PF < 14) issue_w(ks + PF, wf[(ks + PF) % (PF + 1)]);
      if (ks + 1 < 14) {
#pragma unroll
        for (int m = 0; m < 4; ++m) af[(ks + 1) & 1][m] = a_frag(sA, m, ks + 1, lane);
      }
      __builtin_amdgcn_sched_barrier(0);
#pragma unroll
      for (int j = 0; j < 4; ++j)
        if (j < 3 || four) {
#pragma unroll
          for (int m = 0; m < 4; ++m) acc[j][m] = mfma16(wf[ks % (PF + 1)][j], af[ks & 1][m], acc[j][m]);
        }
      __builtin_amdgcn_sched_barrier(0);
    }
  }
  __syncthreads();
  STAMP(5, 3);
  char* sT = smem;
  if (wave < 4) {
#pragma unroll
    for (int j = 0; j < 4; ++j)
      if (j < 3 || four) {
#pragma unroll
        for (int m = 0; m < 4; ++m) stage_tile(sT, ST224, m, nt0 + j, lane, acc[j][m]);
      }
  }
  __syncthreads();
  rsrc_t rg = mk_rsrc(ws + G_OFF, SZ_F);
#pragma unroll
  for (int i = 0; i < 4; ++i) {
    int it = t + NT * i;
    if (it < 64 * 28) {
      int r = it / 28, ch = it - 28 * r;
      u32x4 v = *reinterpret_cast<const u32x4*>(sT + (unsigned)r * ST224 + (unsigned)ch * 16u);
      u32x4 o;
#pragma unroll
      for (int e = 0; e < 4; ++e) o[e] = gelu_pk(v[e]);
      st_sc1(rg, (unsigned)(64 * rb + r) * (FF * 2u) + (unsigned)(224 * g) * 2u + (unsigned)ch * 16u, o);
    }
  }
  signal_block(p, layer * 6 + 4, rb);
  STAMP(5, 5);
}

__device__ __forceinline__ void stage_s6(const Params& p, int layer, int rb, int g, char* smem) {
  STAMP(6, 0);
  STAGE_IDS();
  constexpr int CB = 16384, PFW = 8;
  char* ws = p.ws + (size_t)layer * LAYER_SZ;
  const char* wsrc = p.ws + WI_OFF + WI_4 + ((unsigned)g * 4u + (unsigned)(wave & 3)) * 56u * 1024u + lane * 16;
  bf16x8 wf[PFW + 1];
  if (wave < 4) {
#pragma unroll
    for (int d = 0; d < PFW; ++d) wf[d] = ldw_frag(wsrc + d * 1024);
  }
  unsigned aoff[2], adst[2];
#pragma unroll
  for (int i = 0; i < 2; ++i) {
    int pc = wave + 8 * i;
    int row = 4 * pc + (lane >> 4), c16 = lane & 15;
    aoff[i] = (unsigned)(64 * rb + row) * (FF * 2u) + (unsigned)c16 * 16u;
    adst[i] = (unsigned)row * 256u + (unsigned)((c16 ^ (row & 15)) << 4);
  }
  rsrc_t rg = mk_rsrc(ws + G_OFF, SZ_F);
  constexpr int PFA = 6;
  u32x4 ar[PFA][2];
  auto issue_a = [&](int ch, u32x4 (&r)[2]) {
#pragma unroll
    for (int i = 0; i < 2; ++i) r[i] = ld_sc1(rg, aoff[i] + (unsigned)ch * 256u);
  };
  auto write_a = [&](int ch, const u32x4 (&ra)[2]) {
    char* d = smem + (ch % 3) * CB;
#pragma unroll
    for (int i = 0; i < 2; ++i) *reinterpret_cast<u32x4*>(d + adst[i]) = ra[i];
  };
  wait_blocks(p, layer * 6 + 4, rb, rb);
#pragma unroll
  for (int d = 0; d < PFA; ++d) issue_a(d, ar[d]);
  const int er = t / 7, ech = t - 7 * er;
  const unsigned goff = (unsigned)(64 * rb + er) * ROWB + (unsigned)(GC * g) * 2u + (unsigned)ech * 16u;
  u32x4 x1 = {0u, 0u, 0u, 0u}, x2 = {0u, 0u, 0u, 0u};
  if (t < 448) { x1 = ld_sc1(mk_rsrc(ws + X1_OFF, SZ_D), goff); x2 = ld_sc1(mk_rsrc(ws + X2_OFF, SZ_D), goff); }
  write_a(0, ar[0]);
  issue_a(PFA, ar[0]);
  STAMP(6, 1);
  f32x4 acc[4] = {};
  const int fr = lane & 15, fq = lane >> 4;
#pragma unroll
  for (int ch = 0; ch < 14; ++ch) {
    __syncthreads();
    if (ch + 1 < 14 && !(ABL & 2)) {
      write_a(ch + 1, ar[(ch + 1) % PFA]);
      if (ch + 1 + PFA < 14) issue_a(ch + 1 + PFA, ar[(ch + 1) % PFA]);
    }
    if (wave < 4 && !(ABL & 1)) {
      const char* buf = smem + (ch % 3) * CB;
      bf16x8 af[4][4];
#pragma unroll
      for (int kk = 0; kk < 4; ++kk)
#pragma unroll
        for (int m = 0; m < 4; ++m) af[kk][m] = *reinterpret_cast<const bf16x8*>(buf + (16 * m + fr) * 256 + (((4 * kk + fq) ^ fr) << 4));
      __builtin_amdgcn_sched_barrier(0);
#pragma unroll
      for (int kk = 0; kk < 4; ++kk) {
        const int ks = 4 * ch + kk;
        if (ks + PFW < 56) wf[(ks + PFW) % (PFW + 1)] = ldw_frag(wsrc + (ks + PFW) * 1024);
#pragma unroll
        for (int m = 0; m < 4; ++m) acc[m] = mfma16(wf[ks % (PFW + 1)], af[kk][m], acc[m]);
      }
    }
  }
  __syncthreads();
  STAMP(6, 3);
  char* sT = smem;
  if (wave < 4) {
#pragma unroll
    for (int m = 0; m < 4; ++m) stage_tile(sT, ST64, m, wave, lane, acc[m]);
  }
  __syncthreads();
  if (t < 448) {
    u32x4 v = *reinterpret_cast<const u32x4*>(sT + (unsigned)er * ST64 + (unsigned)ech * 16u);
    u32x4 o;
#pragma unroll
    for (int e = 0; e < 4; ++e) {
      unsigned rr = pack2v(bf_up(x1[e]) + bf_up(v[e]));
      o[e] = pack2v(bf_up(x2[e]) + bf_up(rr));
    }
    if (layer == 0) st_sc1(mk_rsrc(p.ws + XO_OFF, SZ_D), goff, o);
    else __builtin_amdgcn_raw_buffer_store_b128(o, mk_rsrc(p.out, SZ_D), goff, 0, 0);
  }
  if (layer == 0) signal_block(p, 5, rb);
  STAMP(6, 5);
}

__device__ __forceinline__ void stage_s1(const Params& p, int layer, int rb, int g, char* smem) {
  STAMP(1, 0);
  STAGE_IDS();
  constexpr int PF = 6;
  char* sA = smem;
  char* ws = p.ws + (size_t)layer * LAYER_SZ;
  const int nt = wave & 3;
  const char* wsrc = p.ws + WI_OFF + WI_QKV + ((unsigned)g * 12u + (unsigned)nt) * 14u * 1024u + lane * 16;
  bf16x8 wf[PF + 1][3];
  auto issue_w = [&](int ks, bf16x8 (&r)[3]) {
#pragma unroll
    for (int j = 0; j < 3; ++j) r[j] = ldw_frag(wsrc + (j * 4 * 14 + ks) * 1024);
  };
  if (wave < 4) {
#pragma unroll
    for (int d = 0; d < PF; ++d) issue_w(d, wf[d]);
  }
  if (layer > 0) wait_blocks(p, 5, rb - 1, rb);
  {
    rsrc_t rs = layer == 0 ? mk_rsrc(p.x, SZ_D) : mk_rsrc(p.ws + XO_OFF, SZ_D);
    const int c = t & 7;
    u32x4 a0[7], a1[7];
    const int row0 = t >> 3, row1 = 64 + (t >> 3);
    int gr0 = 64 * rb - 16 + row0; gr0 = gr0 < 0 ? 0 : gr0;
#pragma unroll
    for (int j = 0; j < 7; ++j) a0[j] = ld_sc1(rs, (unsigned)gr0 * ROWB + (unsigned)(c + 8 * j) * 16u);
    if (row1 < 80) {
#pragma unroll
      for (int j = 0; j < 7; ++j) a1[j] = ld_sc1(rs, (unsigned)(64 * rb - 16 + row1) * ROWB + (unsigned)(c + 8 * j) * 16u);
    }
    ln_row(a0);
#pragma unroll
    for (int j = 0; j < 7; ++j) *reinterpret_cast<u32x4*>(sA + a_off(row0, c + 8 * j)) = a0[j];
    if (row1 < 80) {
      ln_row(a1);
#pragma unroll
      for (int j = 0; j < 7; ++j) *reinterpret_cast<u32x4*>(sA + a_off(row1, c + 8 * j)) = a1[j];
    }
  }
  STAMP(1, 1);
  __syncthreads();
  f32x4 aq[4] = {}, ak[5] = {}, av[5] = {};
  if (wave < 4) {
    bf16x8 af[2][5];
#pragma unroll
    for (int m = 0; m < 5; ++m) af[0][m] = a_frag(sA, m, 0, lane);
#pragma unroll
    for (int ks = 0; ks < 14; ++ks) {
      if (ks + PF < 14) issue_w(ks + PF, wf[(ks + PF) % (PF + 1)]);
      if (ks + 1 < 14) {
#pragma unroll
        for (int m = 0; m < 5; ++m) af[(ks + 1) & 1][m] = a_frag(sA, m, ks + 1, lane);
      }
      __builtin_amdgcn_sched_barrier(0);
#pragma unroll
      for (int m = 0; m < 5; ++m) {
        if (m > 0) aq[m - 1] = mfma16(wf[ks % (PF + 1)][0], af[ks & 1][m], aq[m - 1]);
        ak[m] = mfma16(wf[ks % (PF + 1)][1], af[ks & 1][m], ak[m]);
        av[m] = mfma16(wf[ks % (PF + 1)][2], af[ks & 1][m], av[m]);
      }
      __builtin_amdgcn_sched_barrier(0);
    }
  }
  __syncthreads();
  STAMP(1, 3);
  char* sQ = smem;
  char* sK = smem + 64 * ST64;
  char* sV = smem + 144 * ST64;
  if (wave < 4) {
#pragma unroll
    for (int m = 0; m < 4; ++m) stage_tile(sQ, ST64, m, nt, lane, aq[m]);
#pragma unroll
    for (int m = 0; m < 5; ++m) { stage_tile(sK, ST64, m, nt, lane, ak[m]); stage_tile(sV, ST64, m, nt, lane, av[m]); }
  }
  __syncthreads();
  STAMP(1, 4);
  char* sO = smem + 224 * ST64;
  if (t < 256) {
    const int sub = t & 1, pr = t >> 1, row = pr >> 1, head = pr & 1;
    const int spos = 64 * rb + row;
    const u32x2* qp = reinterpret_cast<const u32x2*>(sQ + row * ST64 + head * 56);
    u32x2 qv[7];
#pragma unroll
    for (int i = 0; i < 7; ++i) qv[i] = qp[i];
    float lg[4];
#pragma unroll
    for (int ww = 0; ww < 4; ++ww) {
      const u32x2* kp = reinterpret_cast<const u32x2*>(sK + (row + 16 - 2 * (4 * sub + ww)) * ST64 + head * 56);
      float d = 0.f;
#pragma unroll
      for (int i = 0; i < 7; ++i) { u32x2 kv = kp[i]; d = dot2(qv[i][0], kv[0], d); d = dot2(qv[i][1], kv[1], d); }
      lg[ww] = (spos - 2 * (4 * sub + ww) >= 0) ? rbf(rbf(d) * 0.18898223650461362f) : -3.0e38f;
    }
    float mx = fmaxf(fmaxf(lg[0], lg[1]), fmaxf(lg[2], lg[3]));
    mx = fmaxf(mx, __int_as_float(__builtin_amdgcn_update_dpp(0, __float_as_int(mx), 0xB1, 0xF, 0xF, false)));
    float ex[4], sum = 0.f;
#pragma unroll
    for (int ww = 0; ww < 4; ++ww) { ex[ww] = (lg[ww] > -1.0e38f) ? __expf(lg[ww] - mx) : 0.f; sum += ex[ww]; }
    sum += __int_as_float(__builtin_amdgcn_update_dpp(0, __float_as_int(sum), 0xB1, 0xF, 0xF, false));
    const float rs = 1.0f / sum;
    f32x2 o[14];
#pragma unroll
    for (int d = 0; d < 14; ++d) o[d] = f32x2{0.f, 0.f};
#pragma unroll
    for (int ww = 0; ww < 4; ++ww) {
      const float pw = rbf(ex[ww] * rs);
      const f32x2 pw2 = {pw, pw};
      const u32x2* vp = reinterpret_cast<const u32x2*>(sV + (row + 16 - 2 * (4 * sub + ww)) * ST64 + head * 56);
#pragma unroll
      for (int i = 0; i < 7; ++i) {
        u32x2 vv = vp[i];
        const unsigned v0 = vv[0], v1 = vv[1];
        o[2 * i] = pw2 * bf_up(v0) + o[2 * i];
        o[2 * i + 1] = pw2 * bf_up(v1) + o[2 * i + 1];
      }
    }
    unsigned ow[7];
#pragma unroll
    for (int i = 0; i < 7; ++i) {
      f32x2 a = o[i], b = o[7 + i];
      a[0] += __int_as_float(__builtin_amdgcn_update_dpp(0, __float_as_int(a[0]), 0xB1, 0xF, 0xF, false));
      a[1] += __int_as_float(__builtin_amdgcn_update_dpp(0, __float_as_int(a[1]), 0xB1, 0xF, 0xF, false));
      b[0] += __int_as_float(__builtin_amdgcn_update_dpp(0, __float_as_int(b[0]), 0xB1, 0xF, 0xF, false));
      b[1] += __int_as_float(__builtin_amdgcn_update_dpp(0, __float_as_int(b[1]), 0xB1, 0xF, 0xF, false));
      ow[i] = sub == 0 ? pack2v(a) : pack2v(b);
    }
    unsigned* op = reinterpret_cast<unsigned*>(sO + row * 128 + head * 56 + sub * 28);
#pragma unroll
    for (int i = 0; i < 7; ++i) op[i] = ow[i];
  }
  __syncthreads();
  if (t < 448) {
    int r = t / 7, ch = t - 7 * r;
    unsigned goff = (unsigned)(64 * rb + r) * ROWB + (unsigned)(GC * g) * 2u + (unsigned)ch * 16u;
    st_sc1(mk_rsrc(ws + O_OFF, SZ_D), goff, *reinterpret_cast<const u32x4*>(sO + r * 128 + ch * 16));
  }
  signal_block(p, layer * 6 + 0, rb);
  STAMP(1, 5);
}

__global__ __launch_bounds__(256) void prep_kernel(Params p) {
  __shared__ __attribute__((aligned(16))) char sm[4 * 4096];
  const int lane = threadIdx.x & 63, wv = threadIdx.x >> 6;
  const int gw = blockIdx.x * 4 + wv;
  if (blockIdx.x == 0) for (unsigned i = threadIdx.x; i < FLAG_BYTES / 16u; i += 256) reinterpret_cast<u32x4*>(p.ws + FLAG_OFF)[i] = u32x4{0u, 0u, 0u, 0u};
  char* wi = p.ws + WI_OFF;
  char* sw = sm + wv * 4096;
  constexpr int U0 = 8 * 3 * 14, U1 = U0 + 3 * 8 * 14, U2 = U1 + 8 * 4 * 14, U3 = U2 + 8 * 56;
  for (int u = gw; u < U3; u += 1024) {
    const uint16_t* W; int ldw, g, quad, ks, ntiles, nks; unsigned base; int col0, colmax;
    if (u < U0) { g = u / 42; int r = u - g * 42; quad = r / 14; ks = r - quad * 14; W = p.wqkv; ldw = 3 * DM; ntiles = 12; nks = 14; base = WI_QKV;
                  col0 = quad * DM + GC * g; colmax = quad * DM + DM - 8; }
    else if (u < U1) { int q = u - U0, m = q / (8 * 14), r = q - m * (8 * 14); g = r / 14; ks = r - g * 14; quad = 0;
                  W = m == 0 ? p.wo : (m == 1 ? p.w1 : p.w2); ldw = DM; ntiles = 4; nks = 14; base = m == 0 ? WI_O : (m == 1 ? WI_1 : WI_2);
                  col0 = GC * g; colmax = DM - 8; }
    else if (u < U2) { int q = u - U1; g = q / 56; int r = q - g * 56; quad = r / 14; ks = r - quad * 14; W = p.w3; ldw = FF; ntiles = 14; nks = 14; base = WI_3;
                  col0 = 224 * g + 64 * quad; colmax = FF - 8; }
    else { int q = u - U2; g = q / 56; ks = q - g * 56; quad = 0; W = p.w4; ldw = DM; ntiles = 4; nks = 56; base = WI_4; col0 = GC * g; colmax = DM - 8; }
    auto colfn = [&](int nb, int hf) { int c = col0 + 16 * nb + 8 * hf; return c > colmax ? colmax : c; };
#pragma unroll
    for (int pc = 0; pc < 4; ++pc)
      *reinterpret_cast<u32x4*>(sw + pc * 1024 + lane * 16) = *reinterpret_cast<const u32x4*>(W + (size_t)ks * 32 * ldw + w_piece_off<4>(ldw, pc, lane, colfn));
    __builtin_amdgcn_s_waitcnt(0x0);
    const unsigned wl = w_lane_off<4>(lane);
#pragma unroll
    for (int nb = 0; nb < 4; ++nb) {
      int nt = 4 * quad + nb;
      if (nt < ntiles) {
        bf16x8 f = w_frag<4>(sw, 0, nb, wl);
        *reinterpret_cast<bf16x8*>(wi + base + (((unsigned)g * ntiles + nt) * nks + ks) * 1024u + lane * 16) = f;
      }
    }
    __builtin_amdgcn_s_waitcnt(0x0);
  }
}

constexpr unsigned LDS_BYTES = 96 * 1024;

__global__ __launch_bounds__(NT, 2) void persistent_kernel(Params p) {
  extern __shared__ __attribute__((aligned(16))) char smem[];
  const int x = blockIdx.x & 7, i = blockIdx.x >> 3;
  int rb = x * 4 + (i >> 3), g = i & 7;
#pragma unroll
  for (int layer = 0; layer < 2; ++layer) {
    stage_s1(p, layer, rb, g, smem);
    __syncthreads();
    stage_small<2>(p, layer, rb, g, smem);
    __syncthreads();
    stage_small<3>(p, layer, rb, g, smem);
    __syncthreads();
    stage_small<4>(p, layer, rb, g, smem);
    __syncthreads();
    stage_s5(p, layer, rb, g, smem);
    __syncthreads();
    stage_s6(p, layer, rb, g, smem);
    __syncthreads();
  }
}

}

extern "C" void kernel_launch(void* const* d_in, const int* in_sizes, int n_in,
                              void* d_out, int out_size, void* d_ws, size_t ws_size,
                              hipStream_t stream) {
  Params p;
  p.x = (const uint16_t*)d_in[0];
  p.wqkv = (const uint16_t*)d_in[1];
  p.wo = (const uint16_t*)d_in[2];
  p.w1 = (const uint16_t*)d_in[4];
  p.w2 = (const uint16_t*)d_in[5];
  p.w3 = (const uint16_t*)d_in[6];
  p.w4 = (const uint16_t*)d_in[7];
  p.out = (uint16_t*)d_out;
  p.ws = (char*)d_ws;
  (void)hipFuncSetAttribute((const void*)persistent_kernel, hipFuncAttributeMaxDynamicSharedMemorySize, LDS_BYTES);
  prep_kernel<<<256, 256, 0, stream>>>(p);
  persistent_kernel<<<256, NT, LDS_BYTES, stream>>>(p);
}
```
